```python
import torch

def _softmax(x, axis=-1):
    return torch.softmax(x, dim=axis)
def _gelu(x):
    return torch.nn.functional.gelu(x, approximate="tanh")
def _silu(x):
    return torch.nn.functional.silu(x)
def _sigmoid(x):
    return torch.sigmoid(x)
def _layernorm(x, axis=-1, eps=1e-5):
    m = x.mean(dim=axis, keepdim=True)
    v = x.var(dim=axis, keepdim=True, unbiased=False)
    return (x - m) / torch.sqrt(v + eps)
def _scatter_add(tgt, idx, src, axis=0):
    return tgt.clone().index_add_(axis, idx, src)
def _segment_sum(src, seg, n):
    out = torch.zeros((n,) + src.shape[1:], dtype=src.dtype)
    return out.index_add_(0, seg, src)
def _pad(x, pw):
    flat = tuple(v for pair in reversed(pw) for v in pair)
    return torch.nn.functional.pad(x, flat)

kernel_name = 'interleave_layers__sliding_block_cached__dense_activation_mish__decode'

def setup_inputs():

    _w = (lambda a: (lambda t: t.to(torch.bfloat16) if t.is_floating_point() else t)(torch.as_tensor(a)))
    def _A_setup():
        B, Sq, D = 1, 1, 640
        Hq, Hkv, Sk, Dh = 32, 32, 8192, 192
        assert Hq % Hkv == 0, (Hq, Hkv)
        return {
            "x": _w((lambda *s: torch.randn(*s, dtype=torch.bfloat16) * 0.1)(B, Sq, D)),
            "k_cache": _w((lambda *s: torch.randn(*s, dtype=torch.bfloat16) * 0.8)(B, Hkv, Sk, Dh)),
            "v_cache": _w((lambda *s: torch.randn(*s, dtype=torch.bfloat16) * 3.2)(B, Hkv, Sk, Dh)),
            "wq": _w((lambda *s: torch.randn(*s, dtype=torch.bfloat16) * 0.8)(D, Hq * Dh) * (D ** -0.5)),
            "wo": _w((lambda *s: torch.randn(*s, dtype=torch.bfloat16) * 3.2)(Hq * Dh, D) * ((Hq * Dh) ** -0.5)),
        }
    _A_in = _A_setup()

    def _B_setup():
        B, S, D, F = 1, 1, 640, 1536
        return {
            "x": _w((lambda *s: torch.randn(*s, dtype=torch.bfloat16) * 0.1)(B, S, D)),
            "w1": _w((lambda *s: torch.randn(*s, dtype=torch.bfloat16) * 1.6)(D, F) * (D ** -0.5)),
            "b1": _w((lambda *s: torch.randn(*s, dtype=torch.bfloat16) * 0.4)(F)),
            "w2": _w((lambda *s: torch.randn(*s, dtype=torch.bfloat16) * 0.4)(F, D) * (F ** -0.5)),
        }
    _B_in = _B_setup()

    return {
        "A_x": _A_in["x"],
        "A_k_cache": _A_in["k_cache"],
        "A_v_cache": _A_in["v_cache"],
        "A_wq": _A_in["wq"],
        "A_wo": _A_in["wo"],
        "B_x": _B_in["x"],
        "B_w1": _B_in["w1"],
        "B_b1": _B_in["b1"],
        "B_w2": _B_in["w2"],
    }

def reference(A_x, A_k_cache, A_v_cache, A_wq, A_wo, B_x, B_w1, B_b1, B_w2):
    def _A_ref(x, k_cache, v_cache, wq, wo):
        B, Sq, D = x.shape
        Hq, Dh = 32, 192
        Hkv, Sk = k_cache.shape[1], k_cache.shape[2]
        W = min(4096, max(256, Sk // 4))
        rep = Hq // Hkv
        q = (lambda x, *d: x.permute(*d))((x @ wq).reshape(B, Sq, Hq, Dh), 0, 2, 1, 3)
        k = (lambda x, n, axis: torch.repeat_interleave(x, n, dim=axis))(k_cache[:, :, Sk - W:, :], rep, axis=1)
        v = (lambda x, n, axis: torch.repeat_interleave(x, n, dim=axis))(v_cache[:, :, Sk - W:, :], rep, axis=1)
        scores = torch.einsum("bhqd,bhkd->bhqk", q, k) * (Dh ** -0.5)
        p = _softmax(scores, axis=-1)
        o = torch.einsum("bhqk,bhkd->bhqd", p, v)
        return (lambda x, *d: x.permute(*d))(o, 0, 2, 1, 3).reshape(B, Sq, Hq * Dh) @ wo

    def _B_ref(x, w1, b1, w2):
        h = torch.einsum("bsd,df->bsf", x, w1) + b1
        sp = torch.log1p(torch.exp(-torch.abs(h))) + torch.where(h > 0, h, torch.zeros_like(h))
        act = h * torch.tanh(sp)
        return torch.einsum("bsf,fd->bsd", act, w2)

    x = A_x
    x = x + _A_ref(_layernorm(x, axis=-1), A_k_cache, A_v_cache, A_wq, A_wo)
    x = x + _B_ref(_layernorm(x, axis=-1), B_w1, B_b1, B_w2)
    x = x + _A_ref(_layernorm(x, axis=-1), A_k_cache, A_v_cache, A_wq, A_wo)
    x = x + _B_ref(_layernorm(x, axis=-1), B_w1, B_b1, B_w2)
    return x
```

```cpp
#include <hip/hip_runtime.h>
#include <hip/hip_bf16.h>
#include <stdint.h>

typedef unsigned short u16;
constexpr int D = 640, HQ = 32, DH = 192, SK = 8192, W = 2048, F = 1536, NSPLIT = 8;

__device__ __forceinline__ float bf2f(u16 v) { return __uint_as_float(((unsigned)v) << 16); }
__device__ __forceinline__ u16 f2bf(float f) { __hip_bfloat16 b = __float2bfloat16(f); return *reinterpret_cast<u16*>(&b); }
__device__ __forceinline__ float rbf(float f) { return bf2f(f2bf(f)); }

__device__ float block_sum(float v, float* red) {
    for (int o = 32; o > 0; o >>= 1) v += __shfl_xor(v, o);
    int w = threadIdx.x >> 6, nw = blockDim.x >> 6;
    __syncthreads();
    if ((threadIdx.x & 63) == 0) red[w] = v;
    __syncthreads();
    float s = 0; for (int i = 0; i < nw; ++i) s += red[i];
    return s;
}
__device__ void ln_to_lds(const float* x, float* xn, float* red) {
    float s = 0; for (int i = threadIdx.x; i < D; i += blockDim.x) s += x[i];
    float m = block_sum(s, red) / D;
    float v = 0; for (int i = threadIdx.x; i < D; i += blockDim.x) { float d = x[i] - m; v += d * d; }
    v = block_sum(v, red) / D;
    float r = rsqrtf(v + 1e-5f);
    for (int i = threadIdx.x; i < D; i += blockDim.x) xn[i] = rbf((x[i] - m) * r);
    __syncthreads();
}
__global__ void k_init(const u16* x, float* xf) { int i = threadIdx.x + blockIdx.x * blockDim.x; if (i < D) xf[i] = bf2f(x[i]); }

__global__ void k_q(const float* x, const u16* wq, float* q) {
    __shared__ float xn[D], red[8], part[4][64];
    ln_to_lds(x, xn, red);
    int lane = threadIdx.x & 63, w = threadIdx.x >> 6, col = blockIdx.x * 64 + lane;
    float a = 0;
    for (int k = w * 160; k < w * 160 + 160; ++k) a += xn[k] * bf2f(wq[(size_t)k * (HQ * DH) + col]);
    part[w][lane] = a; __syncthreads();
    if (w == 0) q[col] = rbf(part[0][lane] + part[1][lane] + part[2][lane] + part[3][lane]);
}
__global__ void k_attn(const float* q, const u16* kc, const u16* vc, float* po, float* pml) {
    __shared__ float qs[DH], p[256], red[8];
    int h = blockIdx.x / NSPLIT, sp = blockIdx.x % NSPLIT, t = threadIdx.x;
    if (t < DH) qs[t] = q[h * DH + t];
    __syncthreads();
    size_t base = ((size_t)h * SK + (SK - W) + sp * 256) * DH;
    const uint4* kr = (const uint4*)(kc + base + (size_t)t * DH);
    float s = 0;
    for (int c = 0; c < DH / 8; ++c) {
        uint4 v = kr[c]; unsigned u[4] = {v.x, v.y, v.z, v.w};
        for (int j = 0; j < 4; ++j) { s += qs[c * 8 + 2 * j] * __uint_as_float(u[j] << 16) + qs[c * 8 + 2 * j + 1] * __uint_as_float(u[j] & 0xffff0000u); }
    }
    s *= 0.07216878364870322f;
    float m = s; for (int o = 32; o > 0; o >>= 1) m = fmaxf(m, __shfl_xor(m, o));
    if ((t & 63) == 0) red[t >> 6] = m; __syncthreads();
    m = fmaxf(fmaxf(red[0], red[1]), fmaxf(red[2], red[3]));
    float e = __expf(s - m); p[t] = e;
    float l = block_sum(e, red + 4);
    if (t < DH) {
        float o = 0;
        for (int k = 0; k < 256; ++k) o += p[k] * bf2f(vc[base + (size_t)k * DH + t]);
        po[blockIdx.x * DH + t] = o;
    }
    if (t == 0) { pml[blockIdx.x * 2] = m; pml[blockIdx.x * 2 + 1] = l; }
}
__global__ void k_wo(const float* po, const float* pml, const u16* wo, float* yp) {
    __shared__ float o[DH];
    int h = blockIdx.x, t = threadIdx.x;
    if (t < DH) {
        float m = -INFINITY; for (int j = 0; j < NSPLIT; ++j) m = fmaxf(m, pml[(h * NSPLIT + j) * 2]);
        float l = 0, a = 0;
        for (int j = 0; j < NSPLIT; ++j) { float w = __expf(pml[(h * NSPLIT + j) * 2] - m); l += w * pml[(h * NSPLIT + j) * 2 + 1]; a += w * po[(h * NSPLIT + j) * DH + t]; }
        o[t] = rbf(a / l);
    }
    __syncthreads();
    float a = 0;
    for (int k = 0; k < DH; ++k) a += o[k] * bf2f(wo[(size_t)(h * DH + k) * D + t]);
    yp[h * D + t] = a;
}
__global__ void k_res(const float* xin, const float* yp, int np, float* xout, u16* out) {
    int t = threadIdx.x; float a = 0;
    for (int i = 0; i < np; ++i) a += yp[i * D + t];
    float r = rbf(xin[t] + rbf(a)); xout[t] = r; if (out) out[t] = f2bf(r);
}
__global__ void k_mlp(const float* x, const u16* w1, const u16* b1, const u16* w2, float* yp) {
    __shared__ float xn[D], red[8], part[4][64], act[64];
    ln_to_lds(x, xn, red);
    int lane = threadIdx.x & 63, w = threadIdx.x >> 6, col = blockIdx.x * 64 + lane;
    float a = 0;
    for (int k = w * 160; k < w * 160 + 160; ++k) a += xn[k] * bf2f(w1[(size_t)k * F + col]);
    part[w][lane] = a; __syncthreads();
    if (w == 0) {
        float hh = rbf(rbf(part[0][lane] + part[1][lane] + part[2][lane] + part[3][lane]) + bf2f(b1[col]));
        float sp = log1pf(expf(-fabsf(hh))) + fmaxf(hh, 0.f);
        act[lane] = rbf(hh * tanhf(sp));
    }
    __syncthreads();
    for (int c = threadIdx.x; c < D; c += blockDim.x) {
        float s = 0; for (int k = 0; k < 64; ++k) s += act[k] * bf2f(w2[(size_t)(blockIdx.x * 64 + k) * D + c]);
        yp[blockIdx.x * D + c] = s;
    }
}

extern "C" void kernel_launch(void* const* d_in, const int* in_sizes, int n_in, void* d_out, int out_size, void* d_ws, size_t ws_size, hipStream_t stream) {
    const u16* x = (const u16*)d_in[0]; const u16* kc = (const u16*)d_in[1]; const u16* vc = (const u16*)d_in[2];
    const u16* wq = (const u16*)d_in[3]; const u16* wo = (const u16*)d_in[4];
    const u16* w1 = (const u16*)d_in[6]; const u16* b1 = (const u16*)d_in[7]; const u16* w2 = (const u16*)d_in[8];
    float* ws = (float*)d_ws;
    float* xa = ws; float* xb = ws + 1024; float* q = ws + 2048; float* po = q + 8192; float* pml = po + 256 * DH; float* yp = pml + 1024;
    k_init<<<3, 256, 0, stream>>>(x, xa);
    for (int l = 0; l < 2; ++l) {
        k_q<<<96, 256, 0, stream>>>(xa, wq, q);
        k_attn<<<256, 256, 0, stream>>>(q, kc, vc, po, pml);
        k_wo<<<32, 640, 0, stream>>>(po, pml, wo, yp);
        k_res<<<1, 640, 0, stream>>>(xa, yp, 32, xb, nullptr);
        k_mlp<<<24, 256, 0, stream>>>(xb, w1, b1, w2, yp);
        k_res<<<1, 640, 0, stream>>>(xb, yp, 24, xa, l == 1 ? (u16*)d_out : nullptr);
    }
}
```

```cpp
#include <hip/hip_runtime.h>

typedef unsigned u32;
typedef unsigned long long u64;
typedef u32 v4u __attribute__((ext_vector_type(4)));
typedef u32 v2u __attribute__((ext_vector_type(2)));
typedef float v4f __attribute__((ext_vector_type(4)));
typedef float v2f __attribute__((ext_vector_type(2)));
typedef __bf16 hx8 __attribute__((ext_vector_type(8)));
typedef __bf16 hx2 __attribute__((ext_vector_type(2)));
#define GAS __attribute__((address_space(1)))
#define LAS __attribute__((address_space(3)))
#define DEV __device__ __forceinline__

#define V7
constexpr u32 WS_G = 4096;
constexpr u32 WS_D0 = 16384;
constexpr u32 WS_D1 = WS_D0 + 256 * 896;
constexpr u32 WS_CS = WS_D1 + 256 * 896;
constexpr u32 HS = 48 * 4640;
constexpr u32 WS_H1 = WS_CS + 256 * 768, WS_H0 = WS_H1 + HS;
#define WS_H(L) (WS_H0 - (L) * HS)
constexpr u32 H_B = 0;
constexpr u32 CBLK = 48 * 256;
constexpr u32 H_C = 80 * 256;
constexpr u32 H_X = H_C + 8 * CBLK;
constexpr u32 H_A = H_X + 1536;
static_assert(H_A + 32 * 3200 == HS, "hand-off block");
#define BI0 1
#define A_STRIDES const u32 sa = L ? 128 : 16, sp = L ? 16 : 512, sh = L ? 3200 : 4096;
#define B_STRIDES const u32 sb = (L || BI0) ? 256 : 16, sc = (L || BI0) ? 16 : 1280;
constexpr int KC = 252, KD = 134, KA = 349, KB = 448, KE = 583, KQ = 540, KB0 = 150, KS = 202;
#define PF_CODE 22
constexpr u32 SENT = 0xFFFFFFFFu;
#define KM KB0
constexpr int SPIN_CAP = 400000;
#define PS 1
#define GS 24
#define QOFF 0
#define KVN 8
#define GF 8
#define G1 48
#define GW 4
#define KSW 0
#define GM 0
#define BP 1
#define G2 80
#define PFR 0
#define KT -60
#define KR 140
#define KVI 0
#define KVM 0
#define KVW 1

constexpr u32 L_QS = 0;
constexpr u32 L_ZR = 384;
constexpr u32 L_TM = 768;
constexpr u32 L_CNT = 1152;
constexpr u32 L_PM = 1184;
constexpr u32 L_PO = 1280;
constexpr u32 L_WP = 7680;
constexpr u32 L_X1 = 9728;
constexpr u32 L_HP = 11008;
constexpr u32 L_ST = 12032;
constexpr u32 L_CP = 12160;
constexpr u32 L_CS = 14208;
constexpr u32 L_DP = 14976;
constexpr u32 L_WQ = 18176;
constexpr u32 L_END = L_WQ + 4 * 10240;

#define CSTAMP(i) do { } while (0)
#define NOTE(i, v) do { } while (0)
#define LDS_TOTAL L_END
#define STAMP(i) do { } while (0)
#define STAMP_INIT do { } while (0)
#define STAMP_DUMP do { } while (0)

template <class T> DEV T ldnt(const char* base, u32 off) { return __builtin_nontemporal_load((const GAS T*)(base + off)); }
#define LO4(X, d) v4u{plo(X[0] d, X[1] d), plo(X[2] d, X[3] d), plo(X[4] d, X[5] d), plo(X[6] d, X[7] d)}
#define HI4(X, d) v4u{phi(X[0] d, X[1] d), phi(X[2] d, X[3] d), phi(X[4] d, X[5] d), phi(X[6] d, X[7] d)}
template <class T> DEV T ldsr(u32 a) { return *(const LAS T*)(size_t)a; }
template <class T> DEV void ldsw(u32 a, T v) { *(LAS T*)(size_t)a = v; }
DEV float asf(u32 v) { return __builtin_bit_cast(float, v); }
DEV u32 asu(float v) { return __builtin_bit_cast(u32, v); }
DEV u32 canon(u32 v) { return v < 0xFFFFFFFEu ? v : 0xFFFFFFFEu; }
DEV u32 pk2(float a, float b) { v2f t = {a, b}; return __builtin_bit_cast(u32, __builtin_convertvector(t, hx2)); }
DEV u32 plo(u32 a, u32 b) { return __builtin_amdgcn_perm(b, a, 0x05040100u); }
DEV u32 phi(u32 a, u32 b) { return __builtin_amdgcn_perm(b, a, 0x07060302u); }
DEV v4f mm32(v4u a, v4u b, v4f c) { return __builtin_amdgcn_mfma_f32_16x16x32_bf16(__builtin_bit_cast(hx8, a), __builtin_bit_cast(hx8, b), c, 0, 0, 0); }
template <int C> DEV float dppf(float v) { return asf(__builtin_amdgcn_update_dpp(0, asu(v), C, 0xf, 0xf, true)); }
DEV float dot2(u32 a, u32 b, float c) { return __builtin_amdgcn_fdot2_f32_bf16(__builtin_bit_cast(hx2, a), __builtin_bit_cast(hx2, b), c, false); }
#define SELS u32 selo = 0x00003F80u, sehi = 0x3F800000u; asm volatile("" : "+s"(selo), "+s"(sehi));
#define acc_lo(v, c) dot2(v, selo, c)
#define acc_hi(v, c) dot2(v, sehi, c)
DEV float wsum(float v) {
    v += dppf<0xB1>(v); v += dppf<0x4E>(v); v += dppf<0x141>(v); v += dppf<0x140>(v);
    { auto t = __builtin_amdgcn_permlane16_swap(asu(v), asu(v), false, false); v = asf(t[0]) + asf(t[1]); }
    { auto t = __builtin_amdgcn_permlane32_swap(asu(v), asu(v), false, false); v = asf(t[0]) + asf(t[1]); }
    return v;
}
DEV u32 umax(u32 a, u32 b) { return a > b ? a : b; }
DEV u32 mx4(v4u v) { return umax(umax(v.x, v.y), umax(v.z, v.w)); }
DEV void bar() { asm volatile("s_waitcnt lgkmcnt(0)\n\ts_barrier" ::: "memory"); }
DEV u32 now32() { return (u32)__builtin_amdgcn_s_memrealtime(); }
#define UM 0
DEV void until(u32 t) { for (int i = 0; i < 1000 && (int)(now32() - t) < 0; ++i) { } }
DEV void lwait(u32 a, u32 n) { _Pragma("clang loop unroll(disable)") for (int i = 0; i < 8 * SPIN_CAP && *(volatile LAS u32*)(size_t)a < n; ++i) __builtin_amdgcn_s_sleep(1); asm volatile("" ::: "memory"); }
DEV void vm0() { asm volatile("s_waitcnt vmcnt(0)" ::: "memory"); }
#define GLD4(d, off, base, imm) asm volatile("global_load_dwordx4 %0, %1, %2 offset:%3 sc1" : "=&v"(d) : "v"(off), "s"(base), "n"(imm) : "memory")
#define GLD4I(d, off, base, imm) asm volatile("global_load_dwordx4 %0, %1, %2 offset:%3 sc1" : "+v"(d) : "v"(off), "s"(base), "n"(imm) : "memory")
#define GLD2(d, off, base, imm) asm volatile("global_load_dwordx2 %0, %1, %2 offset:%3 sc1" : "=&v"(d) : "v"(off), "s"(base), "n"(imm) : "memory")
#define GLD1(d, off, base, imm) asm volatile("global_load_dword %0, %1, %2 offset:%3 sc1" : "+v"(d) : "v"(off), "s"(base), "n"(imm) : "memory")
#define LDS1(w, i, o, bs, imm) "global_load_dword" w " %" #i ", %" #o ", %" #bs " offset:" #imm " sc1\n\t"
#define MASKED(m, body) "s_mov_b64 exec, %" #m "\n\t" body "s_mov_b64 exec, -1\n\ts_waitcnt vmcnt(0)"
#define BAL(c) __builtin_amdgcn_ballot_w64(c)
#define GST4(off, base, v) asm volatile("global_store_dwordx4 %0, %1, %2 sc1\n\ts_nop 1" :: "v"(off), "v"(v), "s"(base) : "memory")
#define GST2(off, base, v) asm volatile("global_store_dwordx2 %0, %1, %2 sc1" :: "v"(off), "v"(v), "s"(base) : "memory")
#define GST1(off, base, v) asm volatile("global_store_dword %0, %1, %2 sc1" :: "v"(off), "v"(v), "s"(base) : "memory")
#define TAGGED(v, t) v4u{(v.x & 0xFFFFFF00u) | (t & 255u), (v.y & 0xFFFFFF00u) | ((t >> 8) & 255u), (v.z & 0xFFFFFF00u) | ((t >> 16) & 255u), (v.w & 0xFFFFFF00u) | (t >> 24)}
#define UNTAG(v, t) v4u{v.x ^ (t & 255u), v.y ^ ((t >> 8) & 255u), v.z ^ ((t >> 16) & 255u), v.w ^ (t >> 24)}
#define OR4(v) (v.x | v.y | v.z | v.w)
#define SW32(x, y) ({ auto t_ = __builtin_amdgcn_permlane32_swap(asu(x), asu(y), false, false); asf(t_[0]) + asf(t_[1]); })
#define SW16(x, y) ({ auto t_ = __builtin_amdgcn_permlane16_swap(asu(x), asu(y), false, false); asf(t_[0]) + asf(t_[1]); })
DEV u32 gate(u32 a) { u32 t = 0; _Pragma("clang loop unroll(disable)") for (int i = 0; i < 4 * SPIN_CAP; ++i) { t = __builtin_amdgcn_readfirstlane(*(volatile LAS u32*)(size_t)a); if (t) break; __builtin_amdgcn_s_sleep(1); } return t; }
DEV u32 resync(u32 tc, u32 tm) { tm = __builtin_amdgcn_readfirstlane(tm); return (int)(tm - tc) > KT ? tm + KR : tc; }
#define LMAX(msk, addr, v) asm volatile("s_mov_b64 exec, %2\n\tds_max_u32 %0, %1\n\ts_mov_b64 exec, -1" :: "v"(addr), "v"(v), "s"(msk) : "memory")
DEV float fmx(float a, float b) { float d; asm("v_max_f32 %0, %1, %2" : "=v"(d) : "v"(a), "v"(b)); return d; }
DEV float fmx3(float a, float b, float c) { float d; asm("v_max3_f32 %0, %1, %2, %3" : "=v"(d) : "v"(a), "v"(b), "v"(c)); return d; }
#define MAXD(v, ctl) asm("s_nop 1\n\tv_max_f32_dpp %0, %0, %0 " ctl " row_mask:0xf bank_mask:0xf" : "+v"(v))
#define UNDEF(x) asm volatile("" : "=v"(x))
#define SB __builtin_amdgcn_sched_barrier(0)
#define MIX(n, nv) _Pragma("unroll") for (int i_ = 0; i_ < n; ++i_) { __builtin_amdgcn_sched_group_barrier(0x008, 1, 0); __builtin_amdgcn_sched_group_barrier(0x002, nv, 0); }
#define COUNT(old, addr) asm volatile("s_mov_b64 exec, 1\n\tds_add_rtn_u32 %0, %1, %2\n\ts_mov_b64 exec, -1\n\ts_waitcnt lgkmcnt(0)" : "=&v"(old) : "v"(addr), "v"(1u) : "memory")
#define TRY _Pragma("clang loop unroll(disable)") do {
#define RETRY if (m == 0) break; __builtin_amdgcn_s_sleep(PS); } while (++spin < SPIN_CAP);
#define Q_GATHER(LL, w, when) { \
    u32 lo = (lane < 48 ? lane : 48) * 16, dof = lane > 47 + LL ? ((head & 1) ^ 1) * 7168 : (head & 1) * 7168 + lo; \
    const char* db = ws + ((LL ? WS_D1 : WS_D0) + (cl * 16 + (w) * 2) * 896); \
    u32 dw = lb + L_DP + (w) * 800 + lo, ca = lb + L_CNT + 16, qa = lb + L_QS + lane * 8; \
    v4f csum = zf; if (LL) csum = ldsr<v4f>(lb + L_CS + lo); \
    v4u d0, d1, e0, e1; UNDEF(d0); UNDEF(d1); PIN(lo); PIN(dof); PIN(dw); PIN(ca); PIN(qa); PIN(csum); SPIN(db); \
    until(when); \
    u64 m = ~0ull; int spin = 0; \
    TRY \
        asm volatile(MASKED(4, LDS1("x4", 0, 2, 3, 0) LDS1("x4", 1, 2, 3, 896)) : "+v"(d0), "+v"(d1) : "v"(dof), "s"(db), "s"(m) : "memory"); \
        e0 = UNTAG(d0, tl); e1 = UNTAG(d1, tl); \
        m = BAL(((OR4(e0) | OR4(e1)) & 255u) != 0); \
    RETRY \
    if (lane < 49) ldsw<v4f>(dw, __builtin_bit_cast(v4f, e0) + __builtin_bit_cast(v4f, e1)); \
    u32 old; COUNT(old, ca); \
    NOTE(18 + LL, spin + 1); \
    if ((__builtin_amdgcn_readfirstlane(old) & 3) == 3) { \
        v4f s = (ldsr<v4f>(lb + L_DP + lo) + ldsr<v4f>(lb + L_DP + 800 + lo)) + (ldsr<v4f>(lb + L_DP + 1600 + lo) + ldsr<v4f>(lb + L_DP + 2400 + lo)); \
        if (LL) { \
            const float mean = asf(__builtin_amdgcn_readlane(asu(s[0]), 48)) * (1.f / 640.f); \
            const float var = fmaxf(__builtin_fmaf(-mean, mean, asf(__builtin_amdgcn_readlane(asu(s[1]), 48)) * (1.f / 640.f)), 0.f); \
            s = (s - csum * mean) * __builtin_amdgcn_rsqf(var + 1e-5f); \
        } \
        if (lane < 48) ldsw<v2u>(qa, v2u{pk2(s[0], s[1]), pk2(s[2], s[3])}); \
        if (!LL) { asm volatile("s_waitcnt lgkmcnt(0)" ::: "memory"); *(volatile LAS u32*)(size_t)(lb + L_CNT + 20) = 1u; STAMP(3); } \
    } }
#define PIN(x) asm volatile("" : "+v"(x))
#define SPIN(x) asm volatile("" : "+s"(x))
#define FRESH int lane = lane0; PIN(lane); const int g = lane >> 4, r = lane & 15; char* ws = ws0; SPIN(ws); (void)g; (void)r;

__global__ void __launch_bounds__(768, 3)
fused_decode(const char* xin, const char* kc, const char* vc,
             const char* wq, const char* wo, const char* w1,
             const char* b1, const char* w2, char* out, char* ws0)
{
    __shared__ __attribute__((aligned(16))) char smem[LDS_TOTAL];
    const u32 lb = (u32)(size_t)(LAS char*)smem;
    const int lane0 = threadIdx.x & 63;
    const int wave = __builtin_amdgcn_readfirstlane(threadIdx.x >> 6);
    const int b = blockIdx.x;
    const int head = (b & 7) * 4 + (b >> 6), j = (b >> 3) & 7, id = head * 8 + j;
    const int cl = head >> 1, mi = (head & 1) * 8 + j;
    const bool is_wo = mi < 10, is_mlp = mi >= 10 && mi < 13;
    const int mm = cl * 3 + mi - 10;
    const v4f zf = {0.f, 0.f, 0.f, 0.f};
    u32 pf0 = 0, pf1 = 0, pf2 = 0;
    if (wave == 3) {
        u64 pc0;
        asm volatile("s_getpc_b64 %0" : "=s"(pc0));
        asm volatile("global_load_dword %0, %3, %6\n\tglobal_load_dword %1, %4, %6\n\tglobal_load_dword %2, %5, %6" : "=&v"(pf0), "=&v"(pf1), "=&v"(pf2)
                     : "v"((u32)(lane0 * 128)), "v"((u32)(8192 + lane0 * 128)), "v"((u32)(lane0 < PF_CODE ? 16384 + lane0 * 128 : 0)), "s"(pc0 & ~255ull) : "memory");
    }
    STAMP_INIT;
    STAMP(0);
    asm volatile("" : "+s"(xin), "+s"(kc), "+s"(vc), "+s"(wq), "+s"(wo), "+s"(w1), "+s"(b1), "+s"(w2), "+s"(out), "+s"(ws0));
#define TUNE_LOAD

    if (wave >= 4) {
        const int aw = wave - 4;
        v4u K[12], VB[12];
        int L = 0;
#pragma clang loop unroll(disable)
        for (;;) {
            SPIN(L);
            if (L == 0) {
                const size_t kb = ((size_t)head * 8192 + 6144 + j * 256 + aw * 32) * 384;
                u32 ln = lane0; PIN(ln);
                const u32 gg = ln >> 4, rr = ln & 15;
                const u32 vo4 = gg * 384 + rr * 16, vo2 = gg * 384 + 256 + rr * 8;
                const u32 ko = ((rr >> 2) + 4 * (gg >> 1)) * 384 + (gg & 1) * 64 + (rr & 3) * 16;
                v4u V4[8]; v2u V2[8];
                const u32 tq = now32() + QOFF;
                if (aw == 0) __builtin_amdgcn_s_setprio(2);
                __builtin_amdgcn_s_barrier();
                STAMP(6);
#pragma unroll
                for (int i = 0; i < 8; ++i) { V4[i] = ldnt<v4u>(vc + kb + i * 1536, vo4); V2[i] = ldnt<v2u>(vc + kb + i * 1536, vo2); }
                __builtin_amdgcn_sched_barrier(0);
#pragma unroll
                for (int i = 0; i < 12; ++i) K[i] = ldnt<v4u>(kc + kb + (i / 3) * 3072 + (i % 3) * 128, ko);
                __builtin_amdgcn_sched_barrier(0);
                asm volatile("s_mov_b64 exec, 1\n\tds_add_u32 %0, %1\n\ts_mov_b64 exec, -1" :: "v"(lb + L_CNT + 8), "v"(1u) : "memory");
                STAMP(1);
                TUNE_LOAD
                if (aw == 0) {
                    u64 ep;
                    asm volatile("s_load_dwordx2 %0, %1, 0x0\n\ts_waitcnt lgkmcnt(0)" : "=s"(ep) : "s"(ws0) : "memory");
                    const u32 tl = (u32)(ep + 0x9E3779B97F4A7C15ull);
                    const u32 dof = ln > 47 ? ((head & 1) ^ 1) * 7168 : (head & 1) * 7168 + ln * 16;
                    const char* db = ws0 + (WS_D0 + cl * 16 * 896);
                    v4u d[8];
#pragma unroll
                    for (int i = 0; i < 8; ++i) UNDEF(d[i]);
                    v4f s;
                    until(tq + KQ);
                    u64 m = ~0ull; int spin = 0;
                    TRY
                        asm volatile(MASKED(11, LDS1("x4", 0, 8, 9, 0) LDS1("x4", 1, 8, 9, 896) LDS1("x4", 2, 8, 9, 1792) LDS1("x4", 3, 8, 9, 2688)
                                                LDS1("x4", 4, 8, 10, 0) LDS1("x4", 5, 8, 10, 896) LDS1("x4", 6, 8, 10, 1792) LDS1("x4", 7, 8, 10, 2688))
                                     : "+v"(d[0]), "+v"(d[1]), "+v"(d[2]), "+v"(d[3]), "+v"(d[4]), "+v"(d[5]), "+v"(d[6]), "+v"(d[7]) : "v"(dof), "s"(db), "s"(db + 3584), "s"(m) : "memory");
                        u32 bad = 0;
#pragma unroll
                        for (int i = 0; i < 8; ++i) { const v4u e = UNTAG(d[i], tl); bad |= OR4(e); s = i ? s + __builtin_bit_cast(v4f, e) : __builtin_bit_cast(v4f, e); if (i & 1) { PIN(s); PIN(bad); } }
                        m = BAL((bad & 255u) != 0);
                    RETRY
                    if (ln < 48) ldsw<v2u>(lb + L_QS + ln * 8, v2u{pk2(s[0], s[1]), pk2(s[2], s[3])});
                    asm volatile("s_waitcnt lgkmcnt(0)" ::: "memory");
                    *(volatile LAS u32*)(size_t)(lb + L_CNT + 20) = 1u;
                    __builtin_amdgcn_s_setprio(0);
                    STAMP(3);
                    NOTE(18, spin + 1);
                }
#pragma unroll
                for (int d = 0; d < 4; ++d) { VB[2 * d] = LO4(V4, [d]); VB[2 * d + 1] = HI4(V4, [d]); }
#pragma unroll
                for (int d = 0; d < 2; ++d) { VB[8 + 2 * d] = LO4(V2, [d]); VB[9 + 2 * d] = HI4(V2, [d]); }
            }
            FRESH
            u32 qaddr = lb + (((g >> 1) == ((r >> 2) & 1)) ? L_QS + 64 * (g & 1) + 16 * (r & 3) : L_ZR);
            u32 pw1 = lb + L_PO + aw * 800 + 32 * r, pw2 = lb + L_PO + aw * 800 + 512 + 16 * r, pr = lb + L_PO + 16 * (lane < 48 ? lane : 48);
            A_STRIDES
            u32 aoff = (lane >> 1) * sa + (lane & 1) * 8;
            const char* abase = ws + (WS_H(L) + H_A + head * sh + j * sp);
            PIN(qaddr); PIN(pw1); PIN(pw2); PIN(pr); PIN(aoff); SPIN(abase);
            if (L == 0) lwait(lb + L_CNT + 20, 1);
            else {
                if (aw < 4) {
                    const u32 tl = __builtin_amdgcn_readfirstlane(ldsr<u32>(lb + L_CNT + 24));
                    Q_GATHER(1, aw, resync(gate(lb + L_CNT + 12) + KC, *(volatile LAS u32*)(size_t)(lb + L_TM)) + KD)
                }
                bar();
            }
            CSTAMP(2 + 8 * L);
            v4u qb[3];
#pragma unroll
            for (int s = 0; s < 3; ++s) qb[s] = ldsr<v4u>(qaddr + 128 * s);
            SB;
            v4f sa4[4];
#pragma unroll
            for (int s = 0; s < 3; ++s)
#pragma unroll
                for (int t = 0; t < 4; ++t) { sa4[t] = mm32(K[t * 3 + s], qb[s], s ? sa4[t] : zf); SB; }
            float sc[4];
#pragma unroll
            for (int t = 0; t < 4; ++t) { float x = dppf<0x00>(sa4[t][0]); x += dppf<0x55>(sa4[t][1]); x += dppf<0xAA>(sa4[t][2]); x += dppf<0xFF>(sa4[t][3]); sc[t] = x; }
            float m = fmx(fmx3(sc[0], sc[1], sc[2]), sc[3]);
            MAXD(m, "row_ror:4");
            { auto t = __builtin_amdgcn_permlane16_swap(asu(m), asu(m), false, false); m = fmx(asf(t[0]), asf(t[1])); }
            { auto t = __builtin_amdgcn_permlane32_swap(asu(m), asu(m), false, false); m = fmx(asf(t[0]), asf(t[1])); }
            const float cs = 0.07216878364870322f * 1.4426950408889634f;
            const float ms = m * cs;
            ldsw<float>(lb + L_PM + 4 * aw, ms);
            v4u pa;
#pragma unroll
            for (int t = 0; t < 4; ++t) { float p = __builtin_amdgcn_exp2f(__builtin_fmaf(sc[t], cs, -ms)); pa[t] = pk2(p, dppf<0x124>(p)); }
            v4f o[13];
#pragma unroll
            for (int e = 0; e < 12; ++e) o[e] = mm32(pa, VB[e], zf);
            { const v4u ones = {0x3F803F80u, 0x3F803F80u, 0x3F803F80u, 0x3F803F80u}; o[12] = mm32(pa, ones, zf); }
#pragma unroll
            for (int e = 0; e < 13; ++e) PIN(o[e]);
            if (lane < 16) {
                ldsw<v4f>(pw1, v4f{o[0][0], o[1][0], o[2][0], o[3][0]});
                ldsw<v4f>(pw1 + 16, v4f{o[4][0], o[5][0], o[6][0], o[7][0]});
                ldsw<v4f>(pw2, v4f{o[8][0], o[9][0], o[10][0], o[11][0]});
                if (lane == 0) ldsw<float>(pw1 + 768, o[12][0]);
            }
            u32 old;
            COUNT(old, lb + L_CNT);
            old = __builtin_amdgcn_readfirstlane(old);
            CSTAMP(3 + 8 * L);
            if ((old & 7) == 7) {
                const v4f m0 = ldsr<v4f>(lb + L_PM), m1 = ldsr<v4f>(lb + L_PM + 16);
                v4f pv[8];
#pragma unroll
                for (int w = 0; w < 8; ++w) pv[w] = ldsr<v4f>(pr + 800 * w);
                SB;
                const float M = fmx3(fmx3(m0[0], m0[1], m0[2]), fmx3(m0[3], m1[0], m1[1]), fmx(m1[2], m1[3]));
                v4f a0 = pv[0] * __builtin_amdgcn_exp2f(m0[0] - M), a1 = pv[4] * __builtin_amdgcn_exp2f(m1[0] - M);
#pragma unroll
                for (int w = 1; w < 4; ++w) { a0 += pv[w] * __builtin_amdgcn_exp2f(m0[w] - M); a1 += pv[4 + w] * __builtin_amdgcn_exp2f(m1[w] - M); }
                a0 += a1;
                v2u st = {pk2(a0[0], a0[1]), pk2(a0[2], a0[3])};
                if (lane >= 48) st = v2u{asu(M), asu(a0[0])};
                st.x = canon(st.x); st.y = canon(st.y);
                if (lane < 50) GST2(aoff, abase, st);
                STAMP(4 + 8 * L);
            }
            if (L == 0) {
                if (aw == 5 && lane < 48) GST4((u32)(lane * 16), ws + (WS_CS + id * 768), ldsr<v4u>(lb + L_CS + lane * 16));
                if (aw == 3 && is_mlp) {
                    const u32 th = ldsr<u32>(lb + L_CNT + 28);
                    lwait(lb + L_TM + 8, 4);
                    const u32 t = now32() + KM - KC;
                    if (lane < 8) asm volatile("s_nop 4\n\tglobal_store_dwordx2 %0, %1, %2 sc1" :: "v"((u32)(lane * 1024 + WS_G + 256 + mm * 8)), "v"(v2u{t, th}), "s"(ws) : "memory");
                }
                if (aw == 7) {
                    const u32 th = ldsr<u32>(lb + L_CNT + 28);
                    const u32 tq0 = now32();
                    v4u gt = {tq0, lane < 40 ? ~th : th, tq0, th};
                    u64 miss = 0xFFFFFFFFFFull;
                    if (!is_mlp) __builtin_amdgcn_s_sleep(G1); else __builtin_amdgcn_s_sleep(G2);
#pragma clang loop unroll(disable)
                    for (int spin = 0; miss != 0 && spin < SPIN_CAP; ++spin) {
                        const u64 mw = miss & 0xFFFFull;
                        if (!mw) __builtin_amdgcn_s_sleep(GF); else if (is_mlp) __builtin_amdgcn_s_sleep(GW); else __builtin_amdgcn_s_sleep(GS);
                        asm volatile(MASKED(3, LDS1("x4", 0, 1, 2, 0)) : "+v"(gt) : "v"((u32)(lane * 16)), "s"(ws + (WS_G + (b & 7) * 1024)), "s"(mw && (is_mlp || GM) ? mw : miss) : "memory");
                        miss = BAL(gt.y != th || gt.w != th);
                        if ((miss & 0xFFFFull) == 0) { u32 t_; asm volatile("v_mov_b32 %0, %1\n\tds_write_b32 %0, %0" : "=&v"(t_) : "s"(lb + L_TM + 12) : "memory"); }
                    }
                    const u32 t0 = now32();
                    int dt = max((int)(gt.x - t0), (int)(gt.z - t0));
                    dt = max(dt, __builtin_amdgcn_update_dpp(0, dt, 0xB1, 0xf, 0xf, true)); dt = max(dt, __builtin_amdgcn_update_dpp(0, dt, 0x4E, 0xf, 0xf, true));
                    dt = max(dt, __builtin_amdgcn_update_dpp(0, dt, 0x141, 0xf, 0xf, true)); dt = max(dt, __builtin_amdgcn_update_dpp(0, dt, 0x140, 0xf, 0xf, true));
                    dt = max(max(__builtin_amdgcn_readlane(dt, 0), __builtin_amdgcn_readlane(dt, 16)), __builtin_amdgcn_readlane(dt, 32));
                    *(volatile LAS u32*)(size_t)(lb + L_CNT + 12) = (t0 + dt) | 1u;
                    STAMP(5);
                }
            }
            if (!KSW && is_mlp) bar();
            if (L == 0 && (aw == 4 || (aw < 4 && head == 0))) {
                if (!is_mlp) until(gate(lb + L_CNT + 12) + KS); else if (KSW) __builtin_amdgcn_s_sleep(KSW);
                u32 s1 = SENT; PIN(s1);
                const v4u s4 = {s1, s1, s1, s1};
                if (aw == 4) { if (lane < 25) GST4((u32)(lane * 16), ws + (WS_H1 + H_A + id * 400), s4); }
                else {
                    const char* cp = ws + (WS_H1 + H_C + j * CBLK + aw * 3072);
                    GST4((u32)(lane * 16), cp, s4); GST4((u32)(lane * 16 + 1024), cp, s4); GST4((u32)(lane * 16 + 2048), cp, s4);
                }
                vm0();
            }
            if (L == 0 && aw == 6) {
                const u32 tl = ldsr<u32>(lb + L_CNT + 24);
                const u32 co = (lane < 48 ? lane : 47) * 16;
                const char* cb = ws + (WS_CS + head * 8 * 768);
                v4u c[8];
                if (!is_mlp) until(gate(lb + L_CNT + 12) + KS); else if (KSW) __builtin_amdgcn_s_sleep(KSW);
                for (int spin = 0;; ++spin) {
                    GLD4(c[0], co, cb, 0); GLD4(c[1], co, cb, 768); GLD4(c[2], co, cb, 1536); GLD4(c[3], co, cb, 2304);
                    GLD4(c[4], co, cb + 3072, 0); GLD4(c[5], co, cb + 3072, 768); GLD4(c[6], co, cb + 3072, 1536); GLD4(c[7], co, cb + 3072, 2304);
                    vm0();
                    u32 bad = 0;
#pragma unroll
                    for (int i = 0; i < 8; ++i) { PIN(c[i]); c[i] = UNTAG(c[i], tl); bad |= OR4(c[i]); }
                    if (__all((bad & 255u) == 0) || spin > SPIN_CAP) break;
                    __builtin_amdgcn_s_sleep(4);
                }
                v4f cs4 = zf;
#pragma unroll
                for (int i = 0; i < 8; ++i) cs4 += __builtin_bit_cast(v4f, c[i]);
                ldsw<v4f>(lb + L_CS + co, cs4);
                STAMP(7);
            }
            if (KSW && is_mlp) bar();
            if (L == 1) break;
            bar();
            L = 1;
        }
        STAMP_DUMP;
        return;
    }

    const int a = wave;
    u32 tl, th;
    v4u WQ[8], WT[2];
    v4u WA[20];
    float bias = 0.f;
#define CPAIR (u32)(r < 8 ? 32 * (r >> 2) + 8 * g + 2 * (r & 3) : 64 + 4 * g + 2 * (r & 1))
    const int row0 = 160 * (j >> 1) + 64 * (j & 1), row1 = 160 * (j >> 1) + 128 + 16 * (j & 1);
#define XCOL(cp) ((cp) < 64 ? row0 + (cp) : row1 - 64 + (cp))
    u32 xp = 0, tc = 0, tm = 0;
    __builtin_amdgcn_s_setprio(3);
    {
        FRESH
        u64 ep;
        v2u raw[20];
        if (a < 3) {
            const u32 cpair = CPAIR;
            u32 xv[5];
#pragma unroll
            for (int i = 0; i < 5; ++i) xv[i] = *(const GAS u32*)(xin + i * 256 + lane * 4);
            xp = *(const GAS u32*)(xin + XCOL(cpair) * 2);
            const char* wp = wq + ((size_t)row0 * 6144 + head * 192 + a * 64) * 2;
            const char* wt = wq + ((size_t)row1 * 6144 + head * 192 + a * 64) * 2;
#pragma unroll
            for (int i = 0; i < 16; ++i) raw[i] = ldnt<v2u>(wp + ((i >> 3) * 32 + (i & 7)) * 12288, (u32)(g * 8 * 12288 + r * 8));
#pragma unroll
            for (int i = 0; i < 4; ++i) raw[16 + i] = ldnt<v2u>(wt + i * 12288, (u32)(g * 4 * 12288 + r * 8));
            __builtin_amdgcn_sched_barrier(0);
            asm volatile("s_barrier\n\ts_load_dwordx2 %0, %1, 0x0" : "=s"(ep) : "s"(ws) : "memory");
            if (a == 0 && lane < (b < 16 ? 64 : 59)) {
                const u32 off = lane < 25 ? H_A + id * 512 + lane * 16 : lane < 30 ? H_B + (5 * mi + lane - 25) * (BI0 ? 256 : 16) + cl * (BI0 ? 16 : 1280) : H_C + b * 384 + (lane - 30) * 16;
                GST4(lane < 54 ? HS + off : lane < 59 ? H_B + b * 80 + (lane - 54) * 16 : H_X + b * 80 + (lane - 59) * 16, ws + WS_H1, (v4u{SENT, SENT, SENT, SENT}));
            }
            __builtin_amdgcn_sched_barrier(0);
            float sx = 0.f;
#pragma unroll
            for (int i = 0; i < 5; ++i) sx += asf(xv[i] << 16) + asf(xv[i] & 0xFFFF0000u);
            const float mean = wsum(sx) * (1.f / 640.f);
            float sv = 0.f;
#pragma unroll
            for (int i = 0; i < 5; ++i) { const float d0 = asf(xv[i] << 16) - mean, d1 = asf(xv[i] & 0xFFFF0000u) - mean; sv += d0 * d0 + d1 * d1; }
            const float rstd = __builtin_amdgcn_rsqf(wsum(sv) * (1.f / 640.f) + 1e-5f);
            xp = pk2((asf(xp << 16) - mean) * rstd, (asf(xp & 0xFFFF0000u) - mean) * rstd);
        } else {
            if (lane < 25) ldsw<v4u>(lb + L_ZR + 16 * lane, v4u{0u, 0u, 0u, 0u});
            if (lane < 40) { ldsw<v4u>(lb + L_X1 + 32 * lane, v4u{0u, 0u, 0u, 0u}); ldsw<v4u>(lb + L_X1 + 32 * lane + 16, v4u{0u, 0u, 0u, 0u}); }
            if (lane < 2) ldsw<v4u>(lb + L_CNT + 16 * lane, v4u{0u, 0u, 0u, 0u});
            asm volatile("s_waitcnt lgkmcnt(0)\n\ts_barrier\n\ts_load_dwordx2 %0, %1, 0x0" : "=s"(ep) : "s"(ws) : "memory");
#pragma unroll
            for (int i = 0; i < 20; ++i) raw[i] = v2u{0u, 0u};
            vm0(); PIN(pf0); PIN(pf1); PIN(pf2);
        }
#pragma unroll
        for (int s = 0; s < 2; ++s)
#pragma unroll
            for (int d = 0; d < 2; ++d) { WQ[s * 4 + 2 * d] = LO4((raw + 8 * s), [d]); WQ[s * 4 + 2 * d + 1] = HI4((raw + 8 * s), [d]); }
#pragma unroll
        for (int d = 0; d < 2; ++d)
            WT[d] = v4u{plo(raw[16][d], raw[17][d]), plo(raw[18][d], raw[19][d]), phi(raw[16][d], raw[17][d]), phi(raw[18][d], raw[19][d])};
        asm volatile("s_waitcnt lgkmcnt(0)" : "+s"(ep) :: "memory");
        TUNE_LOAD
        ep += 0x9E3779B97F4A7C15ull;
        tl = (u32)ep; th = (u32)(ep >> 32);
        if (a == 3) ldsw<v2u>(lb + L_CNT + 24, v2u{tl, th});
    }
    STAMP(1);
    int L = 0;
    u32 doff = a * 256 + lane0 * 16;
    const char* dbase = ws0 + (WS_D0 + id * 896);
#pragma clang loop unroll(disable)
    for (;;) {
        SPIN(L);
        {
        FRESH
        if (a < 3) {
            v4u A0, A1, A2[2];
#define BC(n) (u32)__builtin_amdgcn_update_dpp(0x3F803F80, (int)xp, 0x150 + (n), 0xf, 0x3, false)
            A0 = v4u{BC(0), BC(1), BC(2), BC(3)}; A1 = v4u{BC(4), BC(5), BC(6), BC(7)}; A2[0] = v4u{BC(8), BC(9), 0u, 0u}; A2[1] = v4u{0u, 0u, A2[0].x, A2[0].y};
#undef BC
            v4f q[4];
#pragma unroll
            for (int c = 0; c < 4; ++c) q[c] = mm32(A0, WQ[c], zf);
#pragma unroll
            for (int c = 0; c < 4; ++c) q[c] = mm32(A1, WQ[4 + c], q[c]);
#pragma unroll
            for (int c = 0; c < 4; ++c) q[c] = mm32(A2[c & 1], WT[c >> 1], q[c]);
            MIX(8, 2)
#pragma unroll
            for (int c = 0; c < 4; ++c) PIN(q[c]);
            v4u qv = v4u{asu(q[0][0]), asu(q[1][0]), asu(q[2][0]), asu(q[3][0])};
            qv = TAGGED(qv, tl);
            if (L == 0) {
                if (g == 2) ldsw<v4u>(lb + L_CS + a * 256 + r * 16, qv);
                vm0();
            }
            if (lane < 16) GST4(doff, dbase, qv);
            STAMP(2 + 8 * L);
        }
        else if (L == 1) {
            const u32 xm = r < 10 ? xp : 0u;
            u32 one2 = 0x3F803F80u; SPIN(one2);
            float s1 = dot2(xm, one2, 0.f), s2 = dot2(xm, xm, 0.f);
            s1 = SW32(s1, s2);
            s1 += dppf<0xB1>(s1); s1 += dppf<0x4E>(s1); s1 += dppf<0x141>(s1); s1 += dppf<0x140>(s1);
            s1 = SW16(s1, s1);
            v4u sv = {asu(s1), (u32)__builtin_amdgcn_readlane(asu(s1), 32), 0u, 0u};
            sv = TAGGED(sv, tl);
            if (lane == 0) GST4(doff, dbase, sv);
        }
        if (L == 0) {
            const u32 qs = lb + L_WQ + a * 10240 + lane * 16;
#pragma unroll
            for (int i = 0; i < 10; ++i) ldsw<v4u>(qs + i * 1024, i < 8 ? WQ[i] : WT[i - 8]);
        }
        }
        if (L == 1) { tc = resync(tc, tm); NOTE(28, tm); bar(); STAMP(12); }
        if (L == 0) {
            FRESH
            __builtin_amdgcn_s_setprio(0);
            lwait(lb + L_CNT + 8, KVN);
            if (mi >= 13 ? KVI : is_mlp ? KVM : 0) lwait(lb + L_CNT, mi >= 13 ? KVI : KVM);
            const u32 gg = g, rr = r;
            if (is_wo) {
                const char* wp = wo + ((size_t)(384 * cl + 96 * a) * 640 + 64 * mi) * 2;
                SPIN(wp);
                v2u raw[24];
#pragma unroll
                for (int i = 0; i < 24; ++i) raw[i] = ldnt<v2u>(wp + ((i >> 3) * 32 + (i & 7)) * 1280, gg * 8 * 1280 + rr * 8);
                __builtin_amdgcn_sched_barrier(0);
                STAMP(7);
#pragma unroll
                for (int s = 0; s < 3; ++s)
#pragma unroll
                    for (int d = 0; d < 2; ++d) { WA[s * 4 + 2 * d] = LO4((raw + 8 * s), [d]); WA[s * 4 + 2 * d + 1] = HI4((raw + 8 * s), [d]); }
#pragma unroll
                for (int e = 12; e < 20; ++e) WA[e] = v4u{0u, 0u, 0u, 0u};
            } else {
                const int mq = is_mlp ? mm : 0;
                const char* p1 = w1 + ((size_t)(160 * a) * 1536 + 32 * mq) * 2;
                const char* p2 = w2 + ((size_t)(32 * mq) * 640 + 160 * a) * 2;
                SPIN(p1); SPIN(p2);
                u32 r1[40]; v4u r2[8]; u32 r3[8];
                u32 braw = 0;
                if (is_mlp) {
                    braw = *(const GAS unsigned short*)(b1 + (32 * mm + 8 * g + (r & 7)) * 2);
#pragma unroll
                    for (int i = 0; i < 8; ++i) { r2[i] = ldnt<v4u>(p2 + i * 1280, gg * 8 * 1280 + rr * 16); r3[i] = ldnt<u32>(p2 + i * 1280 + 256, gg * 8 * 1280 + rr * 4); }
                    __builtin_amdgcn_sched_barrier(0);
#pragma unroll
                    for (int i = 0; i < 40; ++i) r1[i] = ldnt<u32>(p1 + ((i >> 3) * 32 + (i & 7)) * 3072, gg * 8 * 3072 + rr * 4);
                    __builtin_amdgcn_sched_barrier(0);
                    STAMP(7);
                } else {
#pragma unroll
                    for (int i = 0; i < 40; ++i) r1[i] = 0u;
#pragma unroll
                    for (int i = 0; i < 8; ++i) { r2[i] = v4u{0u, 0u, 0u, 0u}; r3[i] = 0u; }
                }
#pragma unroll
                for (int s = 0; s < 5; ++s) { WA[2 * s] = LO4((r1 + 8 * s), ); WA[2 * s + 1] = HI4((r1 + 8 * s), ); }
#pragma unroll
                for (int d = 0; d < 4; ++d) { WA[10 + 2 * d] = LO4(r2, [d]); WA[11 + 2 * d] = HI4(r2, [d]); }
                WA[18] = LO4(r3, ); WA[19] = HI4(r3, );
                bias = asf(braw << 16);
            }
            if (is_mlp) {
                const v4u ones = {0x3F803F80u, 0x3F803F80u, 0x3F803F80u, 0x3F803F80u};
                v4f c0 = zf, c1 = zf;
#pragma unroll
                for (int s = 0; s < 5; ++s) { c0 = mm32(ones, WA[2 * s], c0); c1 = mm32(ones, WA[2 * s + 1], c1); }
                asm volatile("s_mov_b64 exec, 1\n\tds_add_u32 %0, %1\n\ts_mov_b64 exec, -1" :: "v"(lb + L_TM + 8), "v"(1u) : "memory");
                if (g == 0) { ldsw<float>(lb + L_HP + 64 * r + 16 + 4 * a, c0[0]); ldsw<float>(lb + L_HP + 64 * r + 48 + 4 * a, c1[0]); }
            }
            lwait(lb + L_CNT + 20, 1);
            STAMP(4);
        }
        if (is_wo) {
            FRESH
            A_STRIDES
            const char* ab = ws + (WS_H(L) + H_A + (2 * cl + (a >> 1)) * sh);
            u32 ao = ((a & 1) * 12 + g) * sa + r * sp, mo = 24 * sa + ((r + 4 * g) & 7) * sp;
            const char *ab1 = ab + 4 * sa, *ab2 = ab + 8 * sa;
            B_STRIDES
            const char* bbase = ws + (WS_H(L) + H_B + mi * 8 * sb + cl * sc);
            u32 boff = (lane >> 2) * sb + (lane & 3) * 4, ww = lb + L_WP + a * 512 + lane * 16, wr = lb + L_WP + (lane & 31) * 8, ca = lb + L_CNT + 4;
            u32 xr = 0;
            if (cl == 0) {
                const char* xb = L == 0 ? xin : ws + (WS_H1 + H_X);
#pragma clang loop unroll(disable)
                for (int spin = 0; spin < SPIN_CAP; ++spin) {
                    asm volatile("global_load_dword %0, %1, %2 sc1\n\ts_waitcnt vmcnt(0)" : "=&v"(xr) : "v"((u32)(mi * 128 + (lane & 31) * 4)), "s"(xb) : "memory");
                    if (L == 0 || __all(xr != SENT)) break;
                    __builtin_amdgcn_s_sleep(PS);
                }
            }
            v4u av[3] = {}; v2u ml = {};
            v2f xf = {asf(xr << 16), asf(xr & 0xFFFF0000u)};
            PIN(av[0]); PIN(av[1]); PIN(av[2]); PIN(ml); PIN(ao); PIN(mo); PIN(boff); PIN(ww); PIN(wr); PIN(ca); PIN(xf); SPIN(ab); SPIN(ab1); SPIN(ab2); SPIN(bbase);
            if (L == 1) until(tc + KA);
            u64 m = 0x00FF00FF00FF00FFull;
            int spin = 0;
            TRY
                asm volatile(MASKED(7, LDS1("x2", 3, 5, 6, 0) LDS1("x4", 0, 4, 6, 0) LDS1("x4", 1, 4, 8, 0) LDS1("x4", 2, 4, 9, 0))
                             : "+v"(av[0]), "+v"(av[1]), "+v"(av[2]), "+v"(ml) : "v"(ao), "v"(mo), "s"(ab), "s"(m), "s"(ab1), "s"(ab2) : "memory");
                m = BAL(umax(umax(mx4(av[0]), mx4(av[1])), umax(mx4(av[2]), umax(ml.x, ml.y))) == SENT);
            RETRY
            if (L == 0 && mi == 0 && (a & 1) == 0) { const u32 t = now32(); if (lane < 8) GST2((u32)(lane * 1024), ws + (WS_G + cl * 16 + a * 4), (v2u{t, th})); }
            CSTAMP(5 + 8 * L);
            v4f dd[4];
#pragma unroll
            for (int s = 0; s < 3; ++s)
#pragma unroll
                for (int c = 0; c < 4; ++c) dd[c] = mm32(av[s], WA[4 * s + c], s ? dd[c] : zf);
            int Mi = (int)ml.x; Mi ^= (Mi >> 31) & 0x7FFFFFFF;
            Mi = max(Mi, __builtin_amdgcn_update_dpp(0, Mi, 0xB1, 0xf, 0xf, true)); Mi = max(Mi, __builtin_amdgcn_update_dpp(0, Mi, 0x4E, 0xf, 0xf, true)); Mi = max(Mi, __builtin_amdgcn_update_dpp(0, Mi, 0x141, 0xf, 0xf, true));
            Mi ^= (Mi >> 31) & 0x7FFFFFFF;
            const float M = asf((u32)Mi);
            const float e = __builtin_amdgcn_exp2f(asf(ml.x) - M);
            float sl = e * asf(ml.y);
            sl += dppf<0xB1>(sl); sl += dppf<0x4E>(sl); sl += dppf<0x141>(sl);
            const float w = e * __builtin_amdgcn_rcpf(sl);
            const float w0 = dppf<0x150>(w), w1_ = dppf<0x151>(w), w2_ = dppf<0x152>(w), w3 = dppf<0x153>(w);
            MIX(12, 2)
            SB;
            v4f res;
#pragma unroll
            for (int c = 0; c < 4; ++c) res[c] = __builtin_fmaf(dd[c][3], w3, __builtin_fmaf(dd[c][2], w2_, __builtin_fmaf(dd[c][1], w1_, dd[c][0] * w0)));
            if (lane < 32) ldsw<v4f>(ww, res);
            u32 old;
            COUNT(old, ca);
            old = __builtin_amdgcn_readfirstlane(old);
            if ((old & 3) == 3) {
                v2f t[8];
#pragma unroll
                for (int i = 0; i < 8; ++i) t[i] = ldsr<v2f>(wr + i * 256);
                const v2f s = (((t[0] + xf) + t[1]) + (t[2] + t[3])) + ((t[4] + t[5]) + (t[6] + t[7]));
                if (lane < 32) GST1(boff, bbase, canon(pk2(s[0], s[1])));
                STAMP(6 + 8 * L);
            }
            NOTE(20 + L, spin + 1);
        } else if (is_mlp) {
            FRESH
            B_STRIDES
            const char* bb = ws + (WS_H(L) + H_B + a * 20 * sb);
            const char *bb1 = bb + 4 * sb, *bb2 = bb + 8 * sb, *bb3 = bb + 12 * sb, *bb4 = bb + 16 * sb;
            const char* cb = ws + (WS_H(L) + H_C + 2 * a * CBLK + mm * 256);
            u32 bo = g * sb + r * sc, xa = lb + L_X1 + 320 * a + 16 * g, xb = lb + L_X1 + 320 * a + 16 * r, xc = lb + L_X1 + 320 * a + 256 + 4 * r;
            u32 ha = lb + 4 * a + ((g & 1) ? L_ST + 8 * (g & 2) : L_HP + 64 * r + 16 * g), hr = lb + L_HP + 32 * (8 * g + (r & 7)), sr = lb + L_ST;
            u32 co = (r >> 3) * CBLK + 128 * g + 16 * (r & 7);
            v4u ones = {0x3F803F80u, 0x3F803F80u, 0x3F803F80u, 0x3F803F80u};
            u64 fw = BAL(r == mm);
            v4u xs[5];
#pragma unroll
            for (int s = 0; s < 5; ++s) UNDEF(xs[s]);
            PIN(bo); PIN(xa); PIN(xb); PIN(xc); PIN(ha); PIN(hr); PIN(sr); PIN(co); PIN(ones); SPIN(bb); SPIN(bb1); SPIN(bb2); SPIN(bb3); SPIN(bb4); SPIN(cb); SPIN(fw);
            if (L) until(tc + KB); else lwait(lb + L_TM + 12, 1);
            u64 m = ~0ull;
            int spin = 0;
            TRY
                asm volatile(MASKED(8, LDS1("x4", 0, 5, 6, 0) LDS1("x4", 1, 5, 9, 0) LDS1("x4", 2, 5, 10, 0) LDS1("x4", 3, 5, 11, 0) LDS1("x4", 4, 5, 7, 0))
                             : "+v"(xs[0]), "+v"(xs[1]), "+v"(xs[2]), "+v"(xs[3]), "+v"(xs[4]) : "v"(bo), "s"(bb), "s"(bb4), "s"(m), "s"(bb1), "s"(bb2), "s"(bb3) : "memory");
                m = BAL(umax(umax(umax(mx4(xs[0]), mx4(xs[1])), umax(mx4(xs[2]), mx4(xs[3]))), mx4(xs[4])) == SENT);
            if (BP > 1 && m != 0) __builtin_amdgcn_s_sleep(BP);
            RETRY
            CSTAMP(5 + 8 * L);
            const u32 tb = now32();
            v4f h0, h1, sq, sm;
#pragma unroll
            for (int s = 0; s < 5; ++s) {
                sq = mm32(xs[s], xs[s], s ? sq : zf); SB; h0 = mm32(xs[s], WA[2 * s], s ? h0 : zf); SB;
                h1 = mm32(xs[s], WA[2 * s + 1], s ? h1 : zf); SB; sm = mm32(xs[s], ones, s ? sm : zf); SB;
                if (s == 0 && fw) {
                    asm volatile("s_mov_b64 exec, %6\n\tds_write_b128 %0, %1\n\tds_write_b128 %0, %2 offset:64\n\tds_write_b128 %0, %3 offset:128\n\tds_write_b128 %0, %4 offset:192\n\tds_write_b128 %0, %5 offset:256\n\ts_mov_b64 exec, -1"
                                 :: "v"(xa), "v"(xs[0]), "v"(xs[1]), "v"(xs[2]), "v"(xs[3]), "v"(xs[4]), "s"(fw) : "memory");
                    SB;
                }
            }
            const v4u xw = ldsr<v4u>(xb);
            const u32 xt = ldsr<u32>(xc);
            float gs = (sq[0] + sq[1]) + (sq[2] + sq[3]);
            gs += dppf<0xB1>(gs); gs += dppf<0x4E>(gs); gs += dppf<0x141>(gs); gs += dppf<0x140>(gs);
            const float hs = SW32((h0[0] + h0[1]) + (h0[2] + h0[3]), (h1[0] + h1[1]) + (h1[2] + h1[3]));
            const float st = SW32(gs, (sm[0] + sm[1]) + (sm[2] + sm[3]));
            ldsw<float>(ha, SW16(hs, st));
            LMAX(1ull, lb + L_TM + 4, tb);
            bar();
            const v4f hp = ldsr<v4f>(hr), s1 = ldsr<v4f>(sr + 16), s2 = ldsr<v4f>(sr), hc = ldsr<v4f>(hr + 16);
            const u32 tw = ldsr<u32>(lb + L_TM + 4);
            SB;
            v4f y[10];
#pragma unroll
            for (int e = 0; e < 10; ++e) {
                const u32 xv = e < 8 ? xw[e >> 1] : xt;
                UNDEF(y[e]);
                y[e][0] = asf((e & 1) ? xv & 0xFFFF0000u : xv << 16);
                PIN(y[e]);
            }
            SB;
            const float mean = ((s1[0] + s1[1]) + (s1[2] + s1[3])) * (1.f / 640.f);
            const float var = fmaxf(__builtin_fmaf(-mean, mean, ((s2[0] + s2[1]) + (s2[2] + s2[3])) * (1.f / 640.f)), 0.f);
            const float rstd = __builtin_amdgcn_rsqf(var + 1e-5f);
            const float h = __builtin_fmaf(__builtin_fmaf(-mean, (hc[0] + hc[1]) + (hc[2] + hc[3]), (hp[0] + hp[1]) + (hp[2] + hp[3])), rstd, bias);
            const float u = __builtin_amdgcn_exp2f(-fabsf(h) * 1.4426950408889634f);
            const float uu = u * u;
            const float n = __builtin_fmaf(2.f, u, h > 0.f ? 1.f : uu);
            const float act = h * n * __builtin_amdgcn_rcpf(__builtin_fmaf(2.f, h > 0.f ? uu : 1.f, n));
            const u32 ap = pk2(act, dppf<0xB1>(act));
            v4u aa;
            aa[0] = __builtin_amdgcn_update_dpp(0, (int)ap, 0x150, 0xf, 0xf, true); aa[1] = __builtin_amdgcn_update_dpp(0, (int)ap, 0x152, 0xf, 0xf, true);
            aa[2] = __builtin_amdgcn_update_dpp(0, (int)ap, 0x154, 0xf, 0xf, true); aa[3] = __builtin_amdgcn_update_dpp(0, (int)ap, 0x156, 0xf, 0xf, true);
#pragma unroll
            for (int e = 0; e < 10; ++e) y[(e + 8) % 10] = mm32(aa, WA[10 + (e + 8) % 10], y[(e + 8) % 10]);
#pragma unroll
            for (int e = 0; e < 10; ++e) PIN(y[(e + 8) % 10]);
            {
                const u32 yt = pk2(y[8][0], y[9][0]);
                v4u y4 = {pk2(y[0][0], y[1][0]), pk2(y[2][0], y[3][0]), pk2(y[4][0], y[5][0]), pk2(y[6][0], y[7][0])};
                if (g) y4 = v4u{yt, yt, yt, tw};
                y4 = v4u{canon(y4.x), canon(y4.y), canon(y4.z), canon(y4.w)};
                if (lane < 32) GST4(co, cb, y4);
            }
            NOTE(22 + L, spin + 1);
            STAMP(6 + 8 * L);
        }
        if (L == 1 && head != 0) break;
        {
            FRESH
            const u32 cpair = CPAIR;
            const char* cb = ws + (WS_H(L) + H_C + j * CBLK);
            char* dst = L == 0 ? ws + (WS_H1 + H_X) : out;
            u32 co = 3072 * a + lane * 16, cw = lb + L_CP + a * 512 + r * 32 + g * 8, cr = lb + L_CP + (cpair < 64 ? cpair * 4 : cpair * 16 - 768), xo = XCOL(cpair) * 2;
            dbase = ws + (WS_D1 + id * 896);
            u32 qs = lb + L_WQ + a * 10240 + lane * 16;
            SELS
            v4u cv[3];
            UNDEF(cv[0]); UNDEF(cv[1]); UNDEF(cv[2]);
            PIN(co); PIN(qs); PIN(cw); PIN(cr); PIN(xo); PIN(doff); SPIN(cb); SPIN(dst); SPIN(dbase);
            tc = L == 0 ? gate(lb + L_CNT + 12) + KC : tc + KE;
            STAMP(9 + 8 * L);
            NOTE(26 + L, tc);
            until(tc);
            u64 m = ~0ull;
            int spin = 0;
            TRY
#define RQ(i, off) "ds_read_b128 %" #i ", %16 offset:" #off "\n\t"
                asm volatile("s_mov_b64 exec, %15\n\t" LDS1("x4", 0, 13, 14, 0) LDS1("x4", 1, 13, 14, 1024) LDS1("x4", 2, 13, 14, 2048) "s_mov_b64 exec, -1\n\t"
                             RQ(3, 0) RQ(4, 1024) RQ(5, 2048) RQ(6, 3072) RQ(7, 4096) RQ(8, 5120) RQ(9, 6144) RQ(10, 7168) RQ(11, 8192) RQ(12, 9216) "s_waitcnt vmcnt(0) lgkmcnt(0)"
                             : "+v"(cv[0]), "+v"(cv[1]), "+v"(cv[2]), "=&v"(WQ[0]), "=&v"(WQ[1]), "=&v"(WQ[2]), "=&v"(WQ[3]), "=&v"(WQ[4]), "=&v"(WQ[5]), "=&v"(WQ[6]), "=&v"(WQ[7]), "=&v"(WT[0]), "=&v"(WT[1])
                             : "v"(co), "s"(cb), "s"(m), "v"(qs) : "memory");
                m = BAL(umax(umax(mx4(cv[0]), mx4(cv[1])), mx4(cv[2])) == SENT);
            RETRY
            CSTAMP(7 + 8 * L);
            float sl[4], sh[4];
#pragma unroll
            for (int d = 0; d < 4; ++d) {
                sl[d] = asf(cv[0][d] << 16); sh[d] = asf(cv[0][d] & 0xFFFF0000u);
#pragma unroll
                for (int i = 1; i < 3; ++i) { sl[d] = acc_lo(cv[i][d], sl[d]); sh[d] = acc_hi(cv[i][d], sh[d]); }
            }
            const float l0 = SW32(sl[0], sl[2]), l1 = SW32(sl[1], sl[3]), h0 = SW32(sh[0], sh[2]), h1 = SW32(sh[1], sh[3]);
            ldsw<v2f>(cw, v2f{SW16(l0, l1), SW16(h0, h1)});
            LMAX(0x0100010001000100ull, lb + L_TM, umax(umax(cv[0].w, cv[1].w), cv[2].w));
            bar();
            const v2f xs = (ldsr<v2f>(cr) + ldsr<v2f>(cr + 512)) + (ldsr<v2f>(cr + 1024) + ldsr<v2f>(cr + 1536));
            tm = *(volatile LAS u32*)(size_t)(lb + L_TM);
            xp = canon(pk2(xs[0], xs[1]));
            if (head == 0 && a == 3 && r < 10) {
                GST1(xo, dst, xp);
                if (L == 1 && b == 0 && lane == 0) GST2(0u, ws, (v2u{tl, th}));
            }
            if (a == 3 || L == 1) { STAMP(8 + 8 * L); NOTE(24 + L, spin + 1); }
        }
        if (L == 1) break;
        L = 1;
    }
    STAMP_DUMP;
}

extern "C" void kernel_launch(void* const* d_in, const int* in_sizes, int n_in, void* d_out, int out_size,
                              void* d_ws, size_t ws_size, hipStream_t stream)
{
    hipLaunchKernelGGL(fused_decode, dim3(256), dim3(768), 0, stream,
                       (const char*)d_in[0], (const char*)d_in[1], (const char*)d_in[2], (const char*)d_in[3],
                       (const char*)d_in[4], (const char*)d_in[6], (const char*)d_in[7], (const char*)d_in[8],
                       (char*)d_out, (char*)d_ws);
}
```
